```python
import torch

def _softmax(x, axis=-1):
    return torch.softmax(x, dim=axis)
def _gelu(x):
    return torch.nn.functional.gelu(x, approximate="tanh")
def _silu(x):
    return torch.nn.functional.silu(x)
def _sigmoid(x):
    return torch.sigmoid(x)
def _layernorm(x, axis=-1, eps=1e-5):
    m = x.mean(dim=axis, keepdim=True)
    v = x.var(dim=axis, keepdim=True, unbiased=False)
    return (x - m) / torch.sqrt(v + eps)
def _scatter_add(tgt, idx, src, axis=0):
    return tgt.clone().index_add_(axis, idx, src)
def _segment_sum(src, seg, n):
    out = torch.zeros((n,) + src.shape[1:], dtype=src.dtype)
    return out.index_add_(0, seg, src)
def _pad(x, pw):
    flat = tuple(v for pair in reversed(pw) for v in pair)
    return torch.nn.functional.pad(x, flat)

kernel_name = 'standalone__gqa_block_cached__sampling'

def setup_inputs():

    _w = (lambda a: (lambda t: t.to(torch.bfloat16) if t.is_floating_point() else t)(torch.as_tensor(a)))
    B, Sq, D = 32, 32, 1024
    Hq, Hkv, Sk, Dh = 32, 4, 2048, 128
    assert Hq % Hkv == 0, (Hq, Hkv)
    return {
        "x": _w((lambda *s: torch.randn(*s, dtype=torch.bfloat16) * 0.8)(B, Sq, D)),
        "k_cache": _w((lambda *s: torch.randn(*s, dtype=torch.bfloat16) * 0.8)(B, Hkv, Sk, Dh)),
        "v_cache": _w((lambda *s: torch.randn(*s, dtype=torch.bfloat16) * 0.05)(B, Hkv, Sk, Dh)),
        "wq": _w((lambda *s: torch.randn(*s, dtype=torch.bfloat16) * 0.8)(D, Hq * Dh) * (D ** -0.5)),
        "wo": _w((lambda *s: torch.randn(*s, dtype=torch.bfloat16) * 0.05)(Hq * Dh, D) * ((Hq * Dh) ** -0.5)),
    }

def reference(x, k_cache, v_cache, wq, wo):
    B, Sq, D = x.shape
    Hq, Dh = 32, 128
    Hkv = k_cache.shape[1]
    rep = Hq // Hkv
    q = (lambda x, *d: x.permute(*d))((x @ wq).reshape(B, Sq, Hq, Dh), 0, 2, 1, 3)
    k = (lambda x, n, axis: torch.repeat_interleave(x, n, dim=axis))(k_cache, rep, axis=1)
    v = (lambda x, n, axis: torch.repeat_interleave(x, n, dim=axis))(v_cache, rep, axis=1)
    scores = torch.einsum("bhqd,bhkd->bhqk", q, k) * (Dh ** -0.5)
    p = _softmax(scores, axis=-1)
    o = torch.einsum("bhqk,bhkd->bhqd", p, v)
    return (lambda x, *d: x.permute(*d))(o, 0, 2, 1, 3).reshape(B, Sq, Hq * Dh) @ wo
```

```cpp
#include <hip/hip_runtime.h>
#include <hip/hip_bf16.h>
#include <stdint.h>
#include <type_traits>

#define PB   32
#define PSQ  32
#define PD   1024
#define PHQ  32
#define PHKV 4
#define PSK  2048
#define PDH  128
#define PM   (PB * PSQ)
#define PNQ  (PHQ * PDH)

#define QSCALE (0.08838834764831845f * 1.4426950408889634f)

typedef __bf16 bf16_t;
typedef __attribute__((ext_vector_type(8))) __bf16 v8bf;
typedef __attribute__((ext_vector_type(4))) short v4s;
typedef __attribute__((ext_vector_type(8))) short v8s;
typedef __attribute__((ext_vector_type(16))) float v16f;
typedef __attribute__((ext_vector_type(4))) float v4f;
typedef __attribute__((ext_vector_type(4))) unsigned v4u;
typedef __attribute__((ext_vector_type(2))) unsigned v2u;
typedef __attribute__((ext_vector_type(2))) __bf16 v2bf;

#define DI __device__ __forceinline__
#define MFMA32(a, b, c) __builtin_amdgcn_mfma_f32_32x32x16_bf16((a), (b), (c), 0, 0, 0)

DI unsigned pk2(float a, float b) {
    v2bf r; r[0] = (__bf16)a; r[1] = (__bf16)b;
    return __builtin_bit_cast(unsigned, r);
}
DI float fadd_s(float a, float b) { float r; asm("v_add_f32_e32 %0, %1, %2" : "=v"(r) : "v"(a), "v"(b)); return r; }
DI float fmul_s(float a, float b) { float r; asm("v_mul_f32_e32 %0, %1, %2" : "=v"(r) : "v"(a), "v"(b)); return r; }
DI float fmax_s(float a, float b) { float r; asm("v_max_f32_e32 %0, %1, %2" : "=v"(r) : "v"(a), "v"(b)); return r; }
DI float fmax3_s(float a, float b, float c) { float r; asm("v_max3_f32 %0, %1, %2, %3" : "=v"(r) : "v"(a), "v"(b), "v"(c)); return r; }
DI float ffma_s(float a, float b, float c) { float r; asm("v_fma_f32 %0, %1, %2, %3" : "=v"(r) : "v"(a), "v"(b), "v"(c)); return r; }
DI void exp2_sum2(float x0, float x1, float& s0, float& s1, float& p0, float& p1) {
    asm("v_exp_f32_e32 %0, %4\n\tv_exp_f32_e32 %1, %5\n\tv_add_f32_e32 %2, %2, %0\n\tv_add_f32_e32 %3, %3, %1"
        : "=&v"(p0), "=&v"(p1), "+v"(s0), "+v"(s1) : "v"(x0), "v"(x1));
}
DI unsigned exp2_sum_pk(float x0, float x1, float& s0, float& s1) {
    unsigned pk; float p0, p1;
    asm("v_exp_f32_e32 %1, %5\n\tv_exp_f32_e32 %2, %6\n\tv_add_f32_e32 %3, %3, %1\n\tv_add_f32_e32 %4, %4, %2\n\t"
        "v_cvt_pk_bf16_f32 %0, %1, %2"
        : "=v"(pk), "=&v"(p0), "=&v"(p1), "+v"(s0), "+v"(s1) : "v"(x0), "v"(x1));
    return pk;
}
DI float exp2_nop(float x) { float r; asm("v_exp_f32_e32 %0, %1\n\ts_nop 0" : "=v"(r) : "v"(x)); return r; }
DI float fmax_nop(float a, float b) { float r; asm("v_max_f32_e32 %0, %1, %2\n\ts_nop 1" : "=v"(r) : "v"(a), "v"(b)); return r; }

#define SBAR() __builtin_amdgcn_sched_barrier(0)
#define BAR() do { SBAR(); asm volatile("" ::: "memory"); __builtin_amdgcn_s_barrier(); asm volatile("" ::: "memory"); SBAR(); } while (0)
#define BAR_M() BAR()
#define BAR_S() BAR()
#define ACC_DUMP() do {} while (0)
#define WAIT_VM(n) asm volatile("s_waitcnt vmcnt(%0)" :: "n"(n) : "memory")
#define WAIT_LGKM0() asm volatile("s_waitcnt lgkmcnt(0)" ::: "memory")

template <int IMM>
DI void dma16(unsigned m0v, unsigned voff, const char* sbase) {
    asm volatile("s_mov_b32 m0, %0\n\ts_nop 0\n\tglobal_load_lds_dwordx4 %1, %2 offset:%3 nt"
                 :: "s"(m0v), "v"(voff), "s"(sbase), "n"(IMM) : "memory");
}

template <int IMM>
DI void dma16d(unsigned m0v, unsigned voff, const char* sbase) {
    asm volatile("s_mov_b32 m0, %0\n\ts_nop 0\n\tglobal_load_lds_dwordx4 %1, %2 offset:%3"
                 :: "s"(m0v), "v"(voff), "s"(sbase), "n"(IMM) : "memory");
}

#define MPH_LOOKAHEAD 6

struct AttnState {
    v16f oacc[4];
    v16f sacc[2];
    v8bf qf[8];
    v4u pf[4];
    v16f negm;
    unsigned koff[8];
    unsigned voff[4];
    float m, l;
};

template <bool HAS_PV, bool HAS_QK, int VS, int KS, bool FIRST_QK = false>
DI void m_phase(AttnState& st, const char* smem) {
    constexpr int L = MPH_LOOKAHEAD;
    v8bf frag[32];
#pragma unroll
    for (int e = -L; e < 32; ++e) {
        const int f = e + L;
        if (f < 32) {
            const int n = f >> 1;
            if ((f & 1) == 0) {
                if (HAS_PV) {
                    const int s = n >> 2, db = n & 3;
                    v4s lo = __builtin_amdgcn_ds_read_tr16_b64_v4i16(
                        (__attribute__((address_space(3))) v4s*)(smem + st.voff[db] + VS * 16384 + s * 4096));
                    v4s hi = __builtin_amdgcn_ds_read_tr16_b64_v4i16(
                        (__attribute__((address_space(3))) v4s*)(smem + st.voff[db] + VS * 16384 + s * 4096 + 2048));
                    v8s vf = __builtin_shufflevector(lo, hi, 0, 1, 2, 3, 4, 5, 6, 7);
                    frag[f] = __builtin_bit_cast(v8bf, vf);
                }
            } else {
                if (HAS_QK) {
                    const int kb = n >> 3, i = n & 7;
                    frag[f] = *(const v8bf*)(smem + st.koff[i] + KS * 16384 + kb * 8192);
                }
            }
        }
        if (e >= 0) {
            const int n = e >> 1;
            if ((e & 1) == 0) {
                if (HAS_PV) {
                    const int s = n >> 2, db = n & 3;
                    st.oacc[db] = MFMA32(frag[e], __builtin_bit_cast(v8bf, st.pf[s]), st.oacc[db]);
                }
            } else {
                if (HAS_QK) {
                    const int kb = n >> 3, i = n & 7;
                    if (i == 0) {
                        if (FIRST_QK) {
                            v16f z;
#pragma unroll
                            for (int u = 0; u < 16; ++u) z[u] = 0.f;
                            st.sacc[kb] = MFMA32(frag[e], st.qf[i], z);
                        } else {
                            st.sacc[kb] = MFMA32(frag[e], st.qf[i], st.negm);
                        }
                    } else {
                        st.sacc[kb] = MFMA32(frag[e], st.qf[i], st.sacc[kb]);
                    }
                }
            }
        }
        SBAR();
    }
}

#define RESC_THR 8.0f
template <bool FIRST, class F>
DI void s_phase(AttnState& st, F&& dma) {
    float mx0 = fmaxf(fmaxf(st.sacc[0][0], st.sacc[0][1]), st.sacc[0][2]);
    float mx1 = fmaxf(fmaxf(st.sacc[1][0], st.sacc[1][1]), st.sacc[1][2]);
#pragma unroll
    for (int i = 3; i < 15; i += 2) {
        mx0 = fmaxf(fmaxf(mx0, st.sacc[0][i]), st.sacc[0][i + 1]);
        mx1 = fmaxf(fmaxf(mx1, st.sacc[1][i]), st.sacc[1][i + 1]);
    }
    mx0 = fmaxf(fmaxf(mx0, st.sacc[0][15]), st.sacc[1][15]);
    mx0 = fmaxf(mx0, mx1);
    {
        auto pr = __builtin_amdgcn_permlane32_swap(__float_as_uint(mx0), __float_as_uint(mx0), false, false);
        mx0 = fmaxf(__uint_as_float(pr[0]), __uint_as_float(pr[1]));
    }
    if (FIRST) {
#pragma unroll
        for (int kb = 0; kb < 2; ++kb)
#pragma unroll
            for (int i = 0; i < 16; ++i) st.sacc[kb][i] = fadd_s(st.sacc[kb][i], -mx0);
        st.m = mx0;
#pragma unroll
        for (int u = 0; u < 16; ++u) st.negm[u] = -mx0;
    } else if (__builtin_amdgcn_ballot_w64(mx0 > RESC_THR) != 0) {
        const float delta = fmaxf(mx0, 0.f);
        const float alpha = exp2_nop(-delta);
#pragma unroll
        for (int kb = 0; kb < 2; ++kb)
#pragma unroll
            for (int i = 0; i < 16; ++i) st.sacc[kb][i] = fadd_s(st.sacc[kb][i], -delta);
#pragma unroll
        for (int db = 0; db < 4; ++db)
#pragma unroll
            for (int i = 0; i < 16; ++i) st.oacc[db][i] = fmul_s(st.oacc[db][i], alpha);
        st.l = fmul_s(st.l, alpha);
        st.m = fadd_s(st.m, delta);
#pragma unroll
        for (int u = 0; u < 16; ++u) st.negm[u] = -st.m;
    }
    float ls = 0.f;
    SBAR();
#pragma unroll
    for (int kb = 0; kb < 2; ++kb) {
#pragma unroll
        for (int i = 0; i < 16; i += 2) {
            const float p0 = __builtin_amdgcn_exp2f(st.sacc[kb][i]);
            const float p1 = __builtin_amdgcn_exp2f(st.sacc[kb][i + 1]);
            ls += p0; ls += p1;
            st.pf[2 * kb + (i >> 3)][(i & 7) >> 1] = pk2(p0, p1);
            if ((i & 2) != 0) { SBAR(); dma(kb * 4 + (i >> 2)); SBAR(); }
        }
    }
    st.l += ls;
    asm volatile("" : "+v"(st.l));
#pragma unroll
    for (int s = 0; s < 4; ++s) asm volatile("" : "+v"(st.pf[s]));
}

__global__ __launch_bounds__(512, 2) void attn_kernel(const bf16_t* __restrict__ q, const bf16_t* __restrict__ kc,
                                                      const bf16_t* __restrict__ vc, bf16_t* __restrict__ o
                                                      ) {
    __shared__ __attribute__((aligned(16))) char smem[131072];
    const unsigned lds0 = (unsigned)(size_t)(__attribute__((address_space(3))) char*)smem;

    const int tid = threadIdx.x;
    const int lane = tid & 63;
    const int wid = __builtin_amdgcn_readfirstlane(tid >> 6);
    const int grp = wid >> 2;
    const int w = wid & 3;

    const int bid = blockIdx.x;
    const int xcd = bid & 7, j = bid >> 3;
    const int pair = xcd * 16 + (j >> 1);
    const int half = j & 1;
    const int b = pair >> 2, hkv = pair & 3;
    const int hq = hkv * 8 + half * 4 + w;

    const int r = lane & 31, h = lane >> 5;

    AttnState st;
#pragma unroll
    for (int i = 0; i < 8; ++i)
        st.koff[i] = grp * 65536 + r * 256 + 16 * ((2 * i + h) ^ (r & 15));
    {
        const int gi = lane >> 4, i16 = lane & 15, q4 = i16 >> 2, p4 = i16 & 3;
#pragma unroll
        for (int db = 0; db < 4; ++db)
            st.voff[db] = grp * 65536 + 32768 + (4 * (gi >> 1) + q4) * 256 + 64 * (db ^ q4) + 32 * (gi & 1) + 8 * p4;
    }

    const size_t kvbase = ((size_t)(b * PHKV + hkv) * PSK + grp * 1024) * PDH;
    const char* kg = (const char*)(kc + kvbase);
    const char* vg = (const char*)(vc + kvbase);
    unsigned kd[4];
#pragma unroll
    for (int pp = 0; pp < 4; ++pp)
        kd[pp] = (16 * w + (lane >> 4)) * 256 + 16 * ((lane & 15) ^ (4 * pp + (lane >> 4)));
    const unsigned vd = (16 * w + (lane >> 4)) * 256 + 16 * ((lane & 15) ^ ((lane >> 4) << 2));
    const unsigned ldsw = lds0 + grp * 65536 + w * 4096;

    auto dma_piece = [&](int i, const char* kb_, const char* vb_, int KS, int VS) {
        switch (i) {
            case 0: dma16<0>(ldsw + KS * 16384, kd[0], kb_); break;
            case 1: dma16<1024>(ldsw + KS * 16384, kd[1], kb_); break;
            case 2: dma16<2048>(ldsw + KS * 16384, kd[2], kb_); break;
            case 3: dma16<3072>(ldsw + KS * 16384, kd[3], kb_); break;
            case 4: dma16<0>(ldsw + 32768 + VS * 16384, vd, vb_); break;
            case 5: dma16<1024>(ldsw + 32768 + VS * 16384, vd, vb_); break;
            case 6: dma16<2048>(ldsw + 32768 + VS * 16384, vd, vb_); break;
            default: dma16<3072>(ldsw + 32768 + VS * 16384, vd, vb_); break;
        }
    };

    {
        const bf16_t* qp = q + (size_t)(b * PSQ + r) * PNQ + hq * PDH + 8 * h;
#pragma unroll
        for (int i = 0; i < 8; ++i) st.qf[i] = *(const v8bf*)(qp + 16 * i);
    }
    for (int i = 0; i < 4; ++i) dma_piece(i, kg, vg, 0, 0);
    for (int i = 4; i < 8; ++i) dma_piece(i, kg, vg, 0, 0);
    for (int i = 0; i < 4; ++i) dma_piece(i, kg + 16384, vg, 1, 0);

#pragma unroll
    for (int db = 0; db < 4; ++db)
#pragma unroll
        for (int i = 0; i < 16; ++i) st.oacc[db][i] = 0.f;
    st.m = 0.f;
    st.l = 0.f;

    WAIT_VM(8);
    BAR();
    if (grp) BAR();

#define STEP(t, PAR, FIRST, DK, DV, NW, LAST)                                                                     \
    do {                                                                                                          \
        s_phase<FIRST>(st, [&](int i) {                                                                           \
            if ((i < 4 && (DK)) || (i >= 4 && (DV)))                                                              \
                dma_piece(i, kg + (size_t)((t) + 2) * 16384, vg + (size_t)((t) + 1) * 16384, (PAR), 1 - (PAR));  \
        });                                                                                                       \
        SBAR();                                                                                                   \
        WAIT_VM(NW);                                                                                              \
        BAR_S();                                                                                                  \
        m_phase<true, !(LAST), (PAR), 1 - (PAR)>(st, smem);                                                       \
    } while (0)

    m_phase<false, true, 0, 0, true>(st, smem);
    BAR_M();
    STEP(0, 0, true, true, true, 8, false);  BAR_M();
    STEP(1, 1, false, true, true, 8, false); BAR_M();
    for (int t = 2; t < 14; t += 2) {
        STEP(t, 0, false, true, true, 8, false);      BAR_M();
        STEP(t + 1, 1, false, true, true, 8, false);  BAR_M();
    }
    STEP(14, 0, false, false, true, 4, false); BAR_M();
    STEP(15, 1, false, false, false, 0, true);

    {
        auto pr = __builtin_amdgcn_permlane32_swap(__float_as_uint(st.l), __float_as_uint(st.l), false, false);
        st.l = __uint_as_float(pr[0]) + __uint_as_float(pr[1]);
    }
    char* xw = smem + w * 16384;
    float* sx = (float*)(smem + 65536) + w * 128;
    if (grp) {
#pragma unroll
        for (int db = 0; db < 4; ++db)
#pragma unroll
            for (int g4 = 0; g4 < 4; ++g4) {
                v4f t4 = {st.oacc[db][4 * g4], st.oacc[db][4 * g4 + 1], st.oacc[db][4 * g4 + 2], st.oacc[db][4 * g4 + 3]};
                *(v4f*)(xw + (db * 4 + g4) * 1024 + lane * 16) = t4;
            }
        sx[lane] = st.m;
        sx[64 + lane] = st.l;
        WAIT_LGKM0();
        BAR();
        ACC_DUMP();
        return;
    }
    BAR();
    BAR();
    {
        const float mB = sx[lane], lB = sx[64 + lane];
        const float mf = fmaxf(st.m, mB);
        const float aA = __builtin_amdgcn_exp2f(st.m - mf);
        const float aB = __builtin_amdgcn_exp2f(mB - mf);
        const float inv = 1.0f / (st.l * aA + lB * aB);
        const float fA = aA * inv, fB = aB * inv;
        bf16_t* op = o + (size_t)(b * PSQ + r) * PNQ + hq * PDH + 4 * h;
#pragma unroll
        for (int db = 0; db < 4; ++db)
#pragma unroll
            for (int g4 = 0; g4 < 4; ++g4) {
                const v4f ob = *(const v4f*)(xw + (db * 4 + g4) * 1024 + lane * 16);
                v2u pk;
                pk[0] = pk2(st.oacc[db][4 * g4] * fA + ob[0] * fB, st.oacc[db][4 * g4 + 1] * fA + ob[1] * fB);
                pk[1] = pk2(st.oacc[db][4 * g4 + 2] * fA + ob[2] * fB, st.oacc[db][4 * g4 + 3] * fA + ob[3] * fB);
                *(v2u*)(op + 32 * db + 8 * g4) = pk;
            }
    }
    ACC_DUMP();
}

template <int NT, int LDA, int LDB, bool OUT_F32>
__global__ __launch_bounds__(512, 2) void gemm_kernel(const bf16_t* __restrict__ A, const bf16_t* __restrict__ B,
                                                      void* __restrict__ Cout, int ldc, float oscale, int map_mode) {
    static_assert(NT % 4 == 0 && NT >= 8, "k-tiles per workgroup");
    __shared__ __attribute__((aligned(16))) char smem[131072];
    const unsigned lds0 = (unsigned)(size_t)(__attribute__((address_space(3))) char*)smem;
    const int tid = threadIdx.x, lane = tid & 63;
    const int wid = __builtin_amdgcn_readfirstlane(tid >> 6);
    const int grp = wid >> 2, w = wid & 3, wm = w >> 1, wn = w & 1;
    const int r = lane & 31, h = lane >> 5;

    const int bid = blockIdx.x, xcd = bid & 7, j = bid >> 3;
    int tile_m, tile_n, ksp;
    if (map_mode == 0) { tile_n = xcd * 4 + (j & 3); tile_m = j >> 2; ksp = 0; }
    else               { ksp = xcd & 3; tile_n = (xcd >> 2) * 4 + (j & 3); tile_m = j >> 2; }
    const int m0 = tile_m * 128, n0 = tile_n * 128, k0 = ksp * NT * 64;

    unsigned aoff[4], boff[2];
#pragma unroll
    for (int ik = 0; ik < 4; ++ik) aoff[ik] = (wm * 64 + r) * 128 + 16 * ((2 * ik + h) ^ ((r >> 1) & 7));
    {
        const int gi = lane >> 4, i16 = lane & 15, q4 = i16 >> 2, p4 = i16 & 3;
#pragma unroll
        for (int nb = 0; nb < 2; ++nb)
            boff[nb] = 16384 + (8 * (gi >> 1) + q4) * 256 + 64 * ((2 * wn + nb) ^ q4) + 32 * (gi & 1) + 8 * p4;
    }
    unsigned ao[4], bo[4];
#pragma unroll
    for (int pp = 0; pp < 4; ++pp) {
        const int ra = 8 * (4 * w + pp) + (lane >> 3);
        ao[pp] = ra * LDA * 2 + 16 * ((lane & 7) ^ ((4 * pp + (lane >> 4)) & 7)) - 1024 * pp;
        const int rb = 4 * (4 * w + pp) + (lane >> 4);
        bo[pp] = rb * LDB * 2 + 16 * ((lane & 15) ^ ((lane >> 4) << 2)) - 1024 * pp;
    }
    const char* ag = (const char*)(A + (size_t)m0 * LDA + k0);
    const char* bg = (const char*)(B + (size_t)k0 * LDB + n0);
    const unsigned ldw = lds0 + w * 4096;

    auto dma_tile = [&](int t, int slot) {
        const char* pa = ag + (size_t)t * 128;
        const char* pb = bg + (size_t)t * 64 * LDB * 2;
        const unsigned ma = ldw + slot * 32768, mb = ma + 16384;
        dma16d<0>(ma, ao[0], pa); dma16d<1024>(ma, ao[1], pa); dma16d<2048>(ma, ao[2], pa); dma16d<3072>(ma, ao[3], pa);
        dma16d<0>(mb, bo[0], pb); dma16d<1024>(mb, bo[1], pb); dma16d<2048>(mb, bo[2], pb); dma16d<3072>(mb, bo[3], pb);
    };

    v16f acc[2][2];
#pragma unroll
    for (int nb = 0; nb < 2; ++nb)
#pragma unroll
        for (int mb = 0; mb < 2; ++mb)
#pragma unroll
            for (int i = 0; i < 16; ++i) acc[nb][mb][i] = 0.f;

    auto c_phase = [&](auto slc) {
        constexpr int SL = decltype(slc)::value;
        v8bf af[4][2], bf[4][2];
#pragma unroll
        for (int g = -2; g < 4; ++g) {
            const int gl = g + 2;
            if (gl < 4) {
#pragma unroll
                for (int mb = 0; mb < 2; ++mb) af[gl][mb] = *(const v8bf*)(smem + aoff[gl] + SL * 32768 + mb * 4096);
#pragma unroll
                for (int nb = 0; nb < 2; ++nb) {
                    v4s lo = __builtin_amdgcn_ds_read_tr16_b64_v4i16(
                        (__attribute__((address_space(3))) v4s*)(smem + boff[nb] + SL * 32768 + gl * 4096));
                    v4s hi = __builtin_amdgcn_ds_read_tr16_b64_v4i16(
                        (__attribute__((address_space(3))) v4s*)(smem + boff[nb] + SL * 32768 + gl * 4096 + 1024));
                    bf[gl][nb] = __builtin_bit_cast(v8bf, __builtin_shufflevector(lo, hi, 0, 1, 2, 3, 4, 5, 6, 7));
                }
            }
            SBAR();
            if (g >= 0) {
#pragma unroll
                for (int nb = 0; nb < 2; ++nb)
#pragma unroll
                    for (int mb = 0; mb < 2; ++mb) { acc[nb][mb] = MFMA32(bf[g][nb], af[g][mb], acc[nb][mb]); SBAR(); }
            }
        }
    };
    using I0 = std::integral_constant<int, 0>; using I1 = std::integral_constant<int, 1>;
    using I2 = std::integral_constant<int, 2>; using I3 = std::integral_constant<int, 3>;

    dma_tile(grp, grp);
    dma_tile(grp + 2, (grp + 2) & 3);
    WAIT_VM(8);
    BAR();
    if (grp) BAR();

#define GSTEP(SLC, SLN, jj, DM, NW)                                                      \
    do {                                                                                 \
        c_phase(SLC{});                                                                  \
        BAR();                                                                           \
        if (DM) dma_tile(t0 + 2 * (jj) + 4, SLC::value);                                 \
        SBAR();                                                                          \
        WAIT_VM(NW);                                                                     \
        BAR();                                                                           \
    } while (0)

#define GLOOP(SA, SB)                                                                    \
    do {                                                                                 \
        const int t0 = grp;                                                              \
        for (int jj = 0; jj < NT / 2 - 2; jj += 2) {                                     \
            GSTEP(SA, SB, jj, true, 8);                                                  \
            GSTEP(SB, SA, jj + 1, true, 8);                                              \
        }                                                                                \
        GSTEP(SA, SB, 0, false, 0);                                                      \
        c_phase(SB{});                                                                   \
    } while (0)

    if (grp == 0) GLOOP(I0, I2); else GLOOP(I1, I3);

    BAR();
    if (grp == 0) BAR();
    float* xw = (float*)(smem + w * 16384);
    if (grp) {
#pragma unroll
        for (int nb = 0; nb < 2; ++nb)
#pragma unroll
            for (int mb = 0; mb < 2; ++mb)
#pragma unroll
                for (int g4 = 0; g4 < 4; ++g4) {
                    v4f t4 = {acc[nb][mb][4 * g4], acc[nb][mb][4 * g4 + 1], acc[nb][mb][4 * g4 + 2], acc[nb][mb][4 * g4 + 3]};
                    *(v4f*)(xw + ((nb * 2 + mb) * 4 + g4) * 256 + lane * 4) = t4;
                }
        WAIT_LGKM0();
        BAR();
        return;
    }
    BAR();
#pragma unroll
    for (int nb = 0; nb < 2; ++nb)
#pragma unroll
        for (int mb = 0; mb < 2; ++mb) {
            const int m = m0 + wm * 64 + mb * 32 + r;
#pragma unroll
            for (int g4 = 0; g4 < 4; ++g4) {
                const v4f o4 = *(const v4f*)(xw + ((nb * 2 + mb) * 4 + g4) * 256 + lane * 4);
                const int n = n0 + wn * 64 + nb * 32 + 8 * g4 + 4 * h;
                const float c0 = acc[nb][mb][4 * g4] + o4[0], c1 = acc[nb][mb][4 * g4 + 1] + o4[1];
                const float c2 = acc[nb][mb][4 * g4 + 2] + o4[2], c3 = acc[nb][mb][4 * g4 + 3] + o4[3];
                if (OUT_F32) {
                    v4f t4 = {c0, c1, c2, c3};
                    *(v4f*)((float*)Cout + ((size_t)ksp * PM + m) * ldc + n) = t4;
                } else {
                    v2u pk; pk[0] = pk2(c0 * oscale, c1 * oscale); pk[1] = pk2(c2 * oscale, c3 * oscale);
                    *(v2u*)((bf16_t*)Cout + (size_t)m * ldc + n) = pk;
                }
            }
        }
}

__global__ __launch_bounds__(256) void reduce4_kernel(const float* __restrict__ p, bf16_t* __restrict__ out) {
    const size_t i = ((size_t)blockIdx.x * 256 + threadIdx.x) * 8;
    const size_t n = (size_t)PM * PD;
    v4f a0 = *(const v4f*)(p + i), a1 = *(const v4f*)(p + i + 4);
#pragma unroll
    for (int s = 1; s < 4; ++s) { a0 += *(const v4f*)(p + s * n + i); a1 += *(const v4f*)(p + s * n + i + 4); }
    v4u pk; pk[0] = pk2(a0[0], a0[1]); pk[1] = pk2(a0[2], a0[3]); pk[2] = pk2(a1[0], a1[1]); pk[3] = pk2(a1[2], a1[3]);
    *(v4u*)(out + i) = pk;
}

extern "C" void kernel_launch(void* const* d_in, const int* in_sizes, int n_in,
                              void* d_out, int out_size, void* d_ws, size_t ws_size,
                              hipStream_t stream) {
    const bf16_t* x  = (const bf16_t*)d_in[0];
    const bf16_t* kc = (const bf16_t*)d_in[1];
    const bf16_t* vc = (const bf16_t*)d_in[2];
    const bf16_t* wq = (const bf16_t*)d_in[3];
    const bf16_t* wo = (const bf16_t*)d_in[4];
    bf16_t* out = (bf16_t*)d_out;
    bf16_t* qb = (bf16_t*)d_ws;
    bf16_t* ob = qb + (size_t)PM * PNQ;
    float* part = (float*)(ob + (size_t)PM * PNQ);

    gemm_kernel<16, PD, PNQ, false><<<256, 512, 0, stream>>>(x, wq, qb, PNQ, QSCALE, 0);
    attn_kernel<<<256, 512, 0, stream>>>(qb, kc, vc, ob);
    gemm_kernel<16, PNQ, PD, true><<<256, 512, 0, stream>>>(ob, wo, part, PD, 1.0f, 1);
    reduce4_kernel<<<PM * PD / 2048, 256, 0, stream>>>(part, out);
}
```

```cpp
#include <hip/hip_runtime.h>
#include <hip/hip_bf16.h>
#include <stdint.h>
#include <type_traits>

#define PB   32
#define PSQ  32
#define PD   1024
#define PHQ  32
#define PHKV 4
#define PSK  2048
#define PDH  128
#define PM   (PB * PSQ)
#define PNQ  (PHQ * PDH)

#define QSCALE (0.08838834764831845f * 1.4426950408889634f)

typedef __bf16 bf16_t;
typedef __attribute__((ext_vector_type(8))) __bf16 v8bf;
typedef __attribute__((ext_vector_type(4))) short v4s;
typedef __attribute__((ext_vector_type(8))) short v8s;
typedef __attribute__((ext_vector_type(16))) float v16f;
typedef __attribute__((ext_vector_type(4))) float v4f;
typedef __attribute__((ext_vector_type(4))) unsigned v4u;
typedef __attribute__((ext_vector_type(2))) unsigned v2u;
typedef __attribute__((ext_vector_type(2))) __bf16 v2bf;

#define DI __device__ __forceinline__
#define MFMA32(a, b, c) __builtin_amdgcn_mfma_f32_32x32x16_bf16((a), (b), (c), 0, 0, 0)

DI unsigned pk2(float a, float b) {
    v2bf r; r[0] = (__bf16)a; r[1] = (__bf16)b;
    return __builtin_bit_cast(unsigned, r);
}
DI float fadd_s(float a, float b) { float r; asm("v_add_f32_e32 %0, %1, %2" : "=v"(r) : "v"(a), "v"(b)); return r; }
DI float fmul_s(float a, float b) { float r; asm("v_mul_f32_e32 %0, %1, %2" : "=v"(r) : "v"(a), "v"(b)); return r; }
DI float fmax_s(float a, float b) { float r; asm("v_max_f32_e32 %0, %1, %2" : "=v"(r) : "v"(a), "v"(b)); return r; }
DI float fmax3_s(float a, float b, float c) { float r; asm("v_max3_f32 %0, %1, %2, %3" : "=v"(r) : "v"(a), "v"(b), "v"(c)); return r; }
DI float ffma_s(float a, float b, float c) { float r; asm("v_fma_f32 %0, %1, %2, %3" : "=v"(r) : "v"(a), "v"(b), "v"(c)); return r; }
DI void exp2_sum2(float x0, float x1, float& s0, float& s1, float& p0, float& p1) {
    asm("v_exp_f32_e32 %0, %4\n\tv_exp_f32_e32 %1, %5\n\tv_add_f32_e32 %2, %2, %0\n\tv_add_f32_e32 %3, %3, %1"
        : "=&v"(p0), "=&v"(p1), "+v"(s0), "+v"(s1) : "v"(x0), "v"(x1));
}
DI unsigned exp2_sum_pk(float x0, float x1, float& s0, float& s1) {
    unsigned pk; float p0, p1;
    asm("v_exp_f32_e32 %1, %5\n\tv_exp_f32_e32 %2, %6\n\tv_add_f32_e32 %3, %3, %1\n\tv_add_f32_e32 %4, %4, %2\n\t"
        "v_cvt_pk_bf16_f32 %0, %1, %2"
        : "=v"(pk), "=&v"(p0), "=&v"(p1), "+v"(s0), "+v"(s1) : "v"(x0), "v"(x1));
    return pk;
}
DI float exp2_nop(float x) { float r; asm("v_exp_f32_e32 %0, %1\n\ts_nop 0" : "=v"(r) : "v"(x)); return r; }
DI float fmax_nop(float a, float b) { float r; asm("v_max_f32_e32 %0, %1, %2\n\ts_nop 1" : "=v"(r) : "v"(a), "v"(b)); return r; }

#define SBAR() __builtin_amdgcn_sched_barrier(0)
#define BAR() do { SBAR(); asm volatile("" ::: "memory"); __builtin_amdgcn_s_barrier(); asm volatile("" ::: "memory"); SBAR(); } while (0)
#define BAR_M() BAR()
#define BAR_S() BAR()
#define ACC_DUMP() do {} while (0)
#define WAIT_VM(n) asm volatile("s_waitcnt vmcnt(%0)" :: "n"(n) : "memory")
#define WAIT_LGKM0() asm volatile("s_waitcnt lgkmcnt(0)" ::: "memory")

template <int IMM>
DI void dma16(unsigned m0v, unsigned voff, const char* sbase) {
    asm volatile("s_mov_b32 m0, %0\n\ts_nop 0\n\tglobal_load_lds_dwordx4 %1, %2 offset:%3 nt"
                 :: "s"(m0v), "v"(voff), "s"(sbase), "n"(IMM) : "memory");
}

template <int IMM>
DI void dma16d(unsigned m0v, unsigned voff, const char* sbase) {
    asm volatile("s_mov_b32 m0, %0\n\ts_nop 0\n\tglobal_load_lds_dwordx4 %1, %2 offset:%3"
                 :: "s"(m0v), "v"(voff), "s"(sbase), "n"(IMM) : "memory");
}

#define MPH_LOOKAHEAD 6

struct AttnState {
    v16f oacc[4];
    v16f sacc[2];
    v8bf qf[8];
    v4u pf[4];
    v16f negm;
    unsigned koff[8];
    unsigned voff[4];
    float m, l;
};

template <bool HAS_PV, bool HAS_QK, int VS, int KS, bool FIRST_QK = false>
DI void m_phase(AttnState& st, const char* smem) {
    constexpr int L = MPH_LOOKAHEAD;
    v8bf frag[32];
#pragma unroll
    for (int e = -L; e < 32; ++e) {
        const int f = e + L;
        if (f < 32) {
            const int n = f >> 1;
            if ((f & 1) == 0) {
                if (HAS_PV) {
                    const int s = n >> 2, db = n & 3;
                    v4s lo = __builtin_amdgcn_ds_read_tr16_b64_v4i16(
                        (__attribute__((address_space(3))) v4s*)(smem + st.voff[db] + VS * 16384 + s * 4096));
                    v4s hi = __builtin_amdgcn_ds_read_tr16_b64_v4i16(
                        (__attribute__((address_space(3))) v4s*)(smem + st.voff[db] + VS * 16384 + s * 4096 + 2048));
                    v8s vf = __builtin_shufflevector(lo, hi, 0, 1, 2, 3, 4, 5, 6, 7);
                    frag[f] = __builtin_bit_cast(v8bf, vf);
                }
            } else {
                if (HAS_QK) {
                    const int kb = n >> 3, i = n & 7;
                    frag[f] = *(const v8bf*)(smem + st.koff[i] + KS * 16384 + kb * 8192);
                }
            }
        }
        if (e >= 0) {
            const int n = e >> 1;
            if ((e & 1) == 0) {
                if (HAS_PV) {
                    const int s = n >> 2, db = n & 3;
                    st.oacc[db] = MFMA32(frag[e], __builtin_bit_cast(v8bf, st.pf[s]), st.oacc[db]);
                }
            } else {
                if (HAS_QK) {
                    const int kb = n >> 3, i = n & 7;
                    if (i == 0) {
                        if (FIRST_QK) {
                            v16f z;
#pragma unroll
                            for (int u = 0; u < 16; ++u) z[u] = 0.f;
                            st.sacc[kb] = MFMA32(frag[e], st.qf[i], z);
                        } else {
                            st.sacc[kb] = MFMA32(frag[e], st.qf[i], st.negm);
                        }
                    } else {
                        st.sacc[kb] = MFMA32(frag[e], st.qf[i], st.sacc[kb]);
                    }
                }
            }
        }
        SBAR();
    }
}

#define RESC_THR 8.0f
template <bool FIRST, class F>
DI void s_phase(AttnState& st, F&& dma) {
    float mx0 = fmaxf(fmaxf(st.sacc[0][0], st.sacc[0][1]), st.sacc[0][2]);
    float mx1 = fmaxf(fmaxf(st.sacc[1][0], st.sacc[1][1]), st.sacc[1][2]);
#pragma unroll
    for (int i = 3; i < 15; i += 2) {
        mx0 = fmaxf(fmaxf(mx0, st.sacc[0][i]), st.sacc[0][i + 1]);
        mx1 = fmaxf(fmaxf(mx1, st.sacc[1][i]), st.sacc[1][i + 1]);
    }
    mx0 = fmaxf(fmaxf(mx0, st.sacc[0][15]), st.sacc[1][15]);
    mx0 = fmaxf(mx0, mx1);
    {
        auto pr = __builtin_amdgcn_permlane32_swap(__float_as_uint(mx0), __float_as_uint(mx0), false, false);
        mx0 = fmaxf(__uint_as_float(pr[0]), __uint_as_float(pr[1]));
    }
    if (FIRST) {
#pragma unroll
        for (int kb = 0; kb < 2; ++kb)
#pragma unroll
            for (int i = 0; i < 16; ++i) st.sacc[kb][i] = fadd_s(st.sacc[kb][i], -mx0);
        st.m = mx0;
#pragma unroll
        for (int u = 0; u < 16; ++u) st.negm[u] = -mx0;
    } else if (__builtin_amdgcn_ballot_w64(mx0 > RESC_THR) != 0) {
        const float delta = fmaxf(mx0, 0.f);
        const float alpha = exp2_nop(-delta);
#pragma unroll
        for (int kb = 0; kb < 2; ++kb)
#pragma unroll
            for (int i = 0; i < 16; ++i) st.sacc[kb][i] = fadd_s(st.sacc[kb][i], -delta);
#pragma unroll
        for (int db = 0; db < 4; ++db)
#pragma unroll
            for (int i = 0; i < 16; ++i) st.oacc[db][i] = fmul_s(st.oacc[db][i], alpha);
        st.l = fmul_s(st.l, alpha);
        st.m = fadd_s(st.m, delta);
#pragma unroll
        for (int u = 0; u < 16; ++u) st.negm[u] = -st.m;
    }
    float ls = 0.f;
    SBAR();
#pragma unroll
    for (int kb = 0; kb < 2; ++kb) {
#pragma unroll
        for (int i = 0; i < 16; i += 2) {
            const float p0 = __builtin_amdgcn_exp2f(st.sacc[kb][i]);
            const float p1 = __builtin_amdgcn_exp2f(st.sacc[kb][i + 1]);
            ls += p0; ls += p1;
            st.pf[2 * kb + (i >> 3)][(i & 7) >> 1] = pk2(p0, p1);
            if ((i & 2) != 0) { SBAR(); dma(kb * 4 + (i >> 2)); SBAR(); }
        }
    }
    st.l += ls;
    asm volatile("" : "+v"(st.l));
#pragma unroll
    for (int s = 0; s < 4; ++s) asm volatile("" : "+v"(st.pf[s]));
}

__global__ __launch_bounds__(512, 2) void attn_kernel(const bf16_t* __restrict__ q, const bf16_t* __restrict__ kc,
                                                      const bf16_t* __restrict__ vc, bf16_t* __restrict__ o
                                                      ) {
    __shared__ __attribute__((aligned(16))) char smem[131072];
    const unsigned lds0 = (unsigned)(size_t)(__attribute__((address_space(3))) char*)smem;

    const int tid = threadIdx.x;
    const int lane = tid & 63;
    const int wid = __builtin_amdgcn_readfirstlane(tid >> 6);
    const int grp = wid >> 2;
    const int w = wid & 3;

    const int bid = blockIdx.x;
    const int xcd = bid & 7, j = bid >> 3;
    const int pair = xcd * 16 + (j >> 1);
    const int half = j & 1;
    const int b = pair >> 2, hkv = pair & 3;
    const int hq = hkv * 8 + half * 4 + w;

    const int r = lane & 31, h = lane >> 5;

    AttnState st;
#pragma unroll
    for (int i = 0; i < 8; ++i)
        st.koff[i] = grp * 65536 + r * 256 + 16 * ((2 * i + h) ^ (r & 15));
    {
        const int gi = lane >> 4, i16 = lane & 15, q4 = i16 >> 2, p4 = i16 & 3;
#pragma unroll
        for (int db = 0; db < 4; ++db)
            st.voff[db] = grp * 65536 + 32768 + (4 * (gi >> 1) + q4) * 256 + 64 * (db ^ q4) + 32 * (gi & 1) + 8 * p4;
    }

    const size_t kvbase = ((size_t)(b * PHKV + hkv) * PSK + grp * 1024) * PDH;
    const char* kg = (const char*)(kc + kvbase);
    const char* vg = (const char*)(vc + kvbase);
    unsigned kd[4];
#pragma unroll
    for (int pp = 0; pp < 4; ++pp)
        kd[pp] = (16 * w + (lane >> 4)) * 256 + 16 * ((lane & 15) ^ (4 * pp + (lane >> 4)));
    const unsigned vd = (16 * w + (lane >> 4)) * 256 + 16 * ((lane & 15) ^ ((lane >> 4) << 2));
    const unsigned ldsw = lds0 + grp * 65536 + w * 4096;

    auto dma_piece = [&](int i, const char* kb_, const char* vb_, int KS, int VS) {
        switch (i) {
            case 0: dma16<0>(ldsw + KS * 16384, kd[0], kb_); break;
            case 1: dma16<1024>(ldsw + KS * 16384, kd[1], kb_); break;
            case 2: dma16<2048>(ldsw + KS * 16384, kd[2], kb_); break;
            case 3: dma16<3072>(ldsw + KS * 16384, kd[3], kb_); break;
            case 4: dma16<0>(ldsw + 32768 + VS * 16384, vd, vb_); break;
            case 5: dma16<1024>(ldsw + 32768 + VS * 16384, vd, vb_); break;
            case 6: dma16<2048>(ldsw + 32768 + VS * 16384, vd, vb_); break;
            default: dma16<3072>(ldsw + 32768 + VS * 16384, vd, vb_); break;
        }
    };

    {
        const bf16_t* qp = q + (size_t)(b * PSQ + r) * PNQ + hq * PDH + 8 * h;
#pragma unroll
        for (int i = 0; i < 8; ++i) st.qf[i] = *(const v8bf*)(qp + 16 * i);
    }
    for (int i = 0; i < 4; ++i) dma_piece(i, kg, vg, 0, 0);
    for (int i = 4; i < 8; ++i) dma_piece(i, kg, vg, 0, 0);
    for (int i = 0; i < 4; ++i) dma_piece(i, kg + 16384, vg, 1, 0);

#pragma unroll
    for (int db = 0; db < 4; ++db)
#pragma unroll
        for (int i = 0; i < 16; ++i) st.oacc[db][i] = 0.f;
    st.m = 0.f;
    st.l = 0.f;

    WAIT_VM(8);
    BAR();
    if (grp) BAR();

#define STEP(t, PAR, FIRST, DK, DV, NW, LAST)                                                                     \
    do {                                                                                                          \
        s_phase<FIRST>(st, [&](int i) {                                                                           \
            if ((i < 4 && (DK)) || (i >= 4 && (DV)))                                                              \
                dma_piece(i, kg + (size_t)((t) + 2) * 16384, vg + (size_t)((t) + 1) * 16384, (PAR), 1 - (PAR));  \
        });                                                                                                       \
        SBAR();                                                                                                   \
        WAIT_VM(NW);                                                                                              \
        BAR_S();                                                                                                  \
        m_phase<true, !(LAST), (PAR), 1 - (PAR)>(st, smem);                                                       \
    } while (0)

    m_phase<false, true, 0, 0, true>(st, smem);
    BAR_M();
    STEP(0, 0, true, true, true, 8, false);  BAR_M();
    STEP(1, 1, false, true, true, 8, false); BAR_M();
    for (int t = 2; t < 14; t += 2) {
        STEP(t, 0, false, true, true, 8, false);      BAR_M();
        STEP(t + 1, 1, false, true, true, 8, false);  BAR_M();
    }
    STEP(14, 0, false, false, true, 4, false); BAR_M();
    STEP(15, 1, false, false, false, 0, true);

    {
        auto pr = __builtin_amdgcn_permlane32_swap(__float_as_uint(st.l), __float_as_uint(st.l), false, false);
        st.l = __uint_as_float(pr[0]) + __uint_as_float(pr[1]);
    }
    char* xw = smem + w * 16384;
    float* sx = (float*)(smem + 65536) + w * 128;
    if (grp) {
#pragma unroll
        for (int db = 0; db < 4; ++db)
#pragma unroll
            for (int g4 = 0; g4 < 4; ++g4) {
                v4f t4 = {st.oacc[db][4 * g4], st.oacc[db][4 * g4 + 1], st.oacc[db][4 * g4 + 2], st.oacc[db][4 * g4 + 3]};
                *(v4f*)(xw + (db * 4 + g4) * 1024 + lane * 16) = t4;
            }
        sx[lane] = st.m;
        sx[64 + lane] = st.l;
        WAIT_LGKM0();
        BAR();
        ACC_DUMP();
        return;
    }
    BAR();
    BAR();
    {
        const float mB = sx[lane], lB = sx[64 + lane];
        const float mf = fmaxf(st.m, mB);
        const float aA = __builtin_amdgcn_exp2f(st.m - mf);
        const float aB = __builtin_amdgcn_exp2f(mB - mf);
        const float inv = 1.0f / (st.l * aA + lB * aB);
        const float fA = aA * inv, fB = aB * inv;
        char* stg = smem + 65536 + 16384 + w * (32 * 272);
#pragma unroll
        for (int db = 0; db < 4; ++db)
#pragma unroll
            for (int g4 = 0; g4 < 4; ++g4) {
                const v4f ob = *(const v4f*)(xw + (db * 4 + g4) * 1024 + lane * 16);
                v2u pk;
                pk[0] = pk2(st.oacc[db][4 * g4] * fA + ob[0] * fB, st.oacc[db][4 * g4 + 1] * fA + ob[1] * fB);
                pk[1] = pk2(st.oacc[db][4 * g4 + 2] * fA + ob[2] * fB, st.oacc[db][4 * g4 + 3] * fA + ob[3] * fB);
                *(v2u*)(stg + r * 272 + (32 * db + 8 * g4 + 4 * h) * 2) = pk;
            }
        WAIT_LGKM0();
        bf16_t* op = o + (size_t)(b * PSQ) * PNQ + hq * PDH;
#pragma unroll
        for (int it = 0; it < 8; ++it) {
            const int row = it * 4 + (lane >> 4), ch = lane & 15;
            *(v4u*)(op + (size_t)row * PNQ + ch * 8) = *(const v4u*)(stg + row * 272 + ch * 16);
        }
    }
    ACC_DUMP();
}

template <int NT, int LDA, int LDB, bool OUT_F32>
__global__ __launch_bounds__(512, 2) void gemm_kernel(const bf16_t* __restrict__ A, const bf16_t* __restrict__ B,
                                                      void* __restrict__ Cout, int ldc, float oscale, int map_mode) {
    static_assert(NT % 4 == 0 && NT >= 8, "k-tiles per workgroup");
    __shared__ __attribute__((aligned(16))) char smem[131072];
    const unsigned lds0 = (unsigned)(size_t)(__attribute__((address_space(3))) char*)smem;
    const int tid = threadIdx.x, lane = tid & 63;
    const int wid = __builtin_amdgcn_readfirstlane(tid >> 6);
    const int grp = wid >> 2, w = wid & 3, wm = w >> 1, wn = w & 1;
    const int r = lane & 31, h = lane >> 5;

    const int bid = blockIdx.x, xcd = bid & 7, j = bid >> 3;
    int tile_m, tile_n, ksp;
    if (map_mode == 0) { tile_n = xcd * 4 + (j & 3); tile_m = j >> 2; ksp = 0; }
    else               { ksp = xcd & 3; tile_n = (xcd >> 2) * 4 + (j & 3); tile_m = j >> 2; }
    const int m0 = tile_m * 128, n0 = tile_n * 128, k0 = ksp * NT * 64;

    unsigned aoff[4], boff[2];
#pragma unroll
    for (int ik = 0; ik < 4; ++ik) aoff[ik] = (wm * 64 + r) * 128 + 16 * ((2 * ik + h) ^ ((r >> 1) & 7));
    {
        const int gi = lane >> 4, i16 = lane & 15, q4 = i16 >> 2, p4 = i16 & 3;
#pragma unroll
        for (int nb = 0; nb < 2; ++nb)
            boff[nb] = 16384 + (8 * (gi >> 1) + q4) * 256 + 64 * ((2 * wn + nb) ^ q4) + 32 * (gi & 1) + 8 * p4;
    }
    unsigned ao[4], bo[4];
#pragma unroll
    for (int pp = 0; pp < 4; ++pp) {
        const int ra = 8 * (4 * w + pp) + (lane >> 3);
        ao[pp] = ra * LDA * 2 + 16 * ((lane & 7) ^ ((4 * pp + (lane >> 4)) & 7)) - 1024 * pp;
        const int rb = 4 * (4 * w + pp) + (lane >> 4);
        bo[pp] = rb * LDB * 2 + 16 * ((lane & 15) ^ ((lane >> 4) << 2)) - 1024 * pp;
    }
    const char* ag = (const char*)(A + (size_t)m0 * LDA + k0);
    const char* bg = (const char*)(B + (size_t)k0 * LDB + n0);
    const unsigned ldw = lds0 + w * 4096;

    auto dma_tile = [&](int t, int slot) {
        const char* pa = ag + (size_t)t * 128;
        const char* pb = bg + (size_t)t * 64 * LDB * 2;
        const unsigned ma = ldw + slot * 32768, mb = ma + 16384;
        dma16d<0>(ma, ao[0], pa); dma16d<1024>(ma, ao[1], pa); dma16d<2048>(ma, ao[2], pa); dma16d<3072>(ma, ao[3], pa);
        dma16d<0>(mb, bo[0], pb); dma16d<1024>(mb, bo[1], pb); dma16d<2048>(mb, bo[2], pb); dma16d<3072>(mb, bo[3], pb);
    };

    v16f acc[2][2];
#pragma unroll
    for (int nb = 0; nb < 2; ++nb)
#pragma unroll
        for (int mb = 0; mb < 2; ++mb)
#pragma unroll
            for (int i = 0; i < 16; ++i) acc[nb][mb][i] = 0.f;

    auto c_phase = [&](auto slc) {
        constexpr int SL = decltype(slc)::value;
        v8bf af[4][2], bf[4][2];
#pragma unroll
        for (int g = -2; g < 4; ++g) {
            const int gl = g + 2;
            if (gl < 4) {
#pragma unroll
                for (int mb = 0; mb < 2; ++mb) af[gl][mb] = *(const v8bf*)(smem + aoff[gl] + SL * 32768 + mb * 4096);
#pragma unroll
                for (int nb = 0; nb < 2; ++nb) {
                    v4s lo = __builtin_amdgcn_ds_read_tr16_b64_v4i16(
                        (__attribute__((address_space(3))) v4s*)(smem + boff[nb] + SL * 32768 + gl * 4096));
                    v4s hi = __builtin_amdgcn_ds_read_tr16_b64_v4i16(
                        (__attribute__((address_space(3))) v4s*)(smem + boff[nb] + SL * 32768 + gl * 4096 + 1024));
                    bf[gl][nb] = __builtin_bit_cast(v8bf, __builtin_shufflevector(lo, hi, 0, 1, 2, 3, 4, 5, 6, 7));
                }
            }
            SBAR();
            if (g >= 0) {
#pragma unroll
                for (int nb = 0; nb < 2; ++nb)
#pragma unroll
                    for (int mb = 0; mb < 2; ++mb) { acc[nb][mb] = MFMA32(bf[g][nb], af[g][mb], acc[nb][mb]); SBAR(); }
            }
        }
    };
    using I0 = std::integral_constant<int, 0>; using I1 = std::integral_constant<int, 1>;
    using I2 = std::integral_constant<int, 2>; using I3 = std::integral_constant<int, 3>;

    dma_tile(grp, grp);
    dma_tile(grp + 2, (grp + 2) & 3);
    WAIT_VM(8);
    BAR();
    if (grp) BAR();

#define GSTEP(SLC, SLN, jj, DM, NW)                                                      \
    do {                                                                                 \
        c_phase(SLC{});                                                                  \
        BAR();                                                                           \
        if (DM) dma_tile(t0 + 2 * (jj) + 4, SLC::value);                                 \
        SBAR();                                                                          \
        WAIT_VM(NW);                                                                     \
        BAR();                                                                           \
    } while (0)

#define GLOOP(SA, SB)                                                                    \
    do {                                                                                 \
        const int t0 = grp;                                                              \
        for (int jj = 0; jj < NT / 2 - 2; jj += 2) {                                     \
            GSTEP(SA, SB, jj, true, 8);                                                  \
            GSTEP(SB, SA, jj + 1, true, 8);                                              \
        }                                                                                \
        GSTEP(SA, SB, 0, false, 0);                                                      \
        c_phase(SB{});                                                                   \
    } while (0)

    if (grp == 0) GLOOP(I0, I2); else GLOOP(I1, I3);

    BAR();
    if (grp == 0) BAR();
    float* xw = (float*)(smem + w * 16384);
    if (grp) {
#pragma unroll
        for (int nb = 0; nb < 2; ++nb)
#pragma unroll
            for (int mb = 0; mb < 2; ++mb)
#pragma unroll
                for (int g4 = 0; g4 < 4; ++g4) {
                    v4f t4 = {acc[nb][mb][4 * g4], acc[nb][mb][4 * g4 + 1], acc[nb][mb][4 * g4 + 2], acc[nb][mb][4 * g4 + 3]};
                    *(v4f*)(xw + ((nb * 2 + mb) * 4 + g4) * 256 + lane * 4) = t4;
                }
        WAIT_LGKM0();
        BAR();
        return;
    }
    BAR();
    constexpr int RB = OUT_F32 ? 256 : 128;
    constexpr int RS = RB + 16;
    constexpr int LPR = RB / 16;
    char* stg = smem + 65536 + w * (32 * RS);
#pragma unroll
    for (int mb = 0; mb < 2; ++mb) {
#pragma unroll
        for (int nb = 0; nb < 2; ++nb)
#pragma unroll
            for (int g4 = 0; g4 < 4; ++g4) {
                const v4f o4 = *(const v4f*)(xw + ((nb * 2 + mb) * 4 + g4) * 256 + lane * 4);
                const float c0 = acc[nb][mb][4 * g4] + o4[0], c1 = acc[nb][mb][4 * g4 + 1] + o4[1];
                const float c2 = acc[nb][mb][4 * g4 + 2] + o4[2], c3 = acc[nb][mb][4 * g4 + 3] + o4[3];
                const int col = nb * 32 + 8 * g4 + 4 * h;
                if (OUT_F32) {
                    v4f t4 = {c0, c1, c2, c3};
                    *(v4f*)(stg + r * RS + col * 4) = t4;
                } else {
                    v2u pk; pk[0] = pk2(c0 * oscale, c1 * oscale); pk[1] = pk2(c2 * oscale, c3 * oscale);
                    *(v2u*)(stg + r * RS + col * 2) = pk;
                }
            }
        WAIT_LGKM0();
        const int mrow = m0 + wm * 64 + mb * 32;
#pragma unroll
        for (int it = 0; it < 32 * LPR / 64; ++it) {
            const int row = it * (64 / LPR) + lane / LPR, ch = lane % LPR;
            const v4u d = *(const v4u*)(stg + row * RS + ch * 16);
            if (OUT_F32)
                *(v4u*)((float*)Cout + ((size_t)ksp * PM + mrow + row) * ldc + n0 + wn * 64 + ch * 4) = d;
            else
                *(v4u*)((bf16_t*)Cout + (size_t)(mrow + row) * ldc + n0 + wn * 64 + ch * 8) = d;
        }
        WAIT_LGKM0();
    }
}

__global__ __launch_bounds__(256) void reduce4_kernel(const float* __restrict__ p, bf16_t* __restrict__ out) {
    const size_t i = ((size_t)blockIdx.x * 256 + threadIdx.x) * 8;
    const size_t n = (size_t)PM * PD;
    v4f a0 = *(const v4f*)(p + i), a1 = *(const v4f*)(p + i + 4);
#pragma unroll
    for (int s = 1; s < 4; ++s) { a0 += *(const v4f*)(p + s * n + i); a1 += *(const v4f*)(p + s * n + i + 4); }
    v4u pk; pk[0] = pk2(a0[0], a0[1]); pk[1] = pk2(a0[2], a0[3]); pk[2] = pk2(a1[0], a1[1]); pk[3] = pk2(a1[2], a1[3]);
    *(v4u*)(out + i) = pk;
}

extern "C" void kernel_launch(void* const* d_in, const int* in_sizes, int n_in,
                              void* d_out, int out_size, void* d_ws, size_t ws_size,
                              hipStream_t stream) {
    const bf16_t* x  = (const bf16_t*)d_in[0];
    const bf16_t* kc = (const bf16_t*)d_in[1];
    const bf16_t* vc = (const bf16_t*)d_in[2];
    const bf16_t* wq = (const bf16_t*)d_in[3];
    const bf16_t* wo = (const bf16_t*)d_in[4];
    bf16_t* out = (bf16_t*)d_out;
    bf16_t* qb = (bf16_t*)d_ws;
    bf16_t* ob = qb + (size_t)PM * PNQ;
    float* part = (float*)(ob + (size_t)PM * PNQ);

    gemm_kernel<16, PD, PNQ, false><<<256, 512, 0, stream>>>(x, wq, qb, PNQ, QSCALE, 0);
    attn_kernel<<<256, 512, 0, stream>>>(qb, kc, vc, ob);
    gemm_kernel<16, PNQ, PD, true><<<256, 512, 0, stream>>>(ob, wo, part, PD, 1.0f, 1);
    reduce4_kernel<<<PM * PD / 2048, 256, 0, stream>>>(part, out);
}
```

```cpp
#include <hip/hip_runtime.h>
#include <hip/hip_bf16.h>
#include <stdint.h>
#include <type_traits>

#define PB   32
#define PSQ  32
#define PD   1024
#define PHQ  32
#define PHKV 4
#define PSK  2048
#define PDH  128
#define PM   (PB * PSQ)
#define PNQ  (PHQ * PDH)

#define QSCALE (0.08838834764831845f * 1.4426950408889634f)

typedef __bf16 bf16_t;
typedef __attribute__((ext_vector_type(8))) __bf16 v8bf;
typedef __attribute__((ext_vector_type(4))) short v4s;
typedef __attribute__((ext_vector_type(8))) short v8s;
typedef __attribute__((ext_vector_type(16))) float v16f;
typedef __attribute__((ext_vector_type(4))) float v4f;
typedef __attribute__((ext_vector_type(4))) unsigned v4u;
typedef __attribute__((ext_vector_type(2))) unsigned v2u;
typedef __attribute__((ext_vector_type(2))) __bf16 v2bf;

#define DI __device__ __forceinline__
#define MFMA32(a, b, c) __builtin_amdgcn_mfma_f32_32x32x16_bf16((a), (b), (c), 0, 0, 0)

DI unsigned pk2(float a, float b) {
    v2bf r; r[0] = (__bf16)a; r[1] = (__bf16)b;
    return __builtin_bit_cast(unsigned, r);
}
DI float fadd_s(float a, float b) { float r; asm("v_add_f32_e32 %0, %1, %2" : "=v"(r) : "v"(a), "v"(b)); return r; }
DI float fmul_s(float a, float b) { float r; asm("v_mul_f32_e32 %0, %1, %2" : "=v"(r) : "v"(a), "v"(b)); return r; }
DI float exp2_nop(float x) { float r; asm("v_exp_f32_e32 %0, %1\n\ts_nop 0" : "=v"(r) : "v"(x)); return r; }

#define SBAR() __builtin_amdgcn_sched_barrier(0)
#define BAR() do { SBAR(); asm volatile("" ::: "memory"); __builtin_amdgcn_s_barrier(); asm volatile("" ::: "memory"); SBAR(); } while (0)
#define BAR_M() BAR()
#define BAR_S() BAR()
#define WAIT_VM(n) asm volatile("s_waitcnt vmcnt(%0)" :: "n"(n) : "memory")
#define WAIT_LGKM0() asm volatile("s_waitcnt lgkmcnt(0)" ::: "memory")

DI const char* uniform_ptr(const char* p) {
    const unsigned long long v = (unsigned long long)p;
    const unsigned lo = __builtin_amdgcn_readfirstlane((unsigned)v), hi = __builtin_amdgcn_readfirstlane((unsigned)(v >> 32));
    return (const char*)(((unsigned long long)hi << 32) | lo);
}
template <int IMM>
DI void dma16(unsigned m0v, unsigned voff, const char* sbase) {
    asm volatile("s_mov_b32 m0, %0\n\ts_nop 0\n\tglobal_load_lds_dwordx4 %1, %2 offset:%3 nt"
                 :: "s"(__builtin_amdgcn_readfirstlane(m0v)), "v"(voff), "s"(uniform_ptr(sbase)), "n"(IMM) : "memory");
}

template <int IMM>
DI void dma16d(unsigned m0v, unsigned voff, const char* sbase) {
    asm volatile("s_mov_b32 m0, %0\n\ts_nop 0\n\tglobal_load_lds_dwordx4 %1, %2 offset:%3"
                 :: "s"(__builtin_amdgcn_readfirstlane(m0v)), "v"(voff), "s"(uniform_ptr(sbase)), "n"(IMM) : "memory");
}

#define MPH_LOOKAHEAD 6

struct AttnState {
    v16f oacc[4];
    v16f sacc[2];
    v8bf qf[8];
    v4u pf[4];
    v16f negm;
    unsigned koff[8];
    unsigned voff[4];
    float m, l;
};

template <bool HAS_PV, bool HAS_QK, int VS, int KS, bool FIRST_QK = false>
DI void m_phase(AttnState& st, const char* smem) {
    constexpr int L = MPH_LOOKAHEAD;
    v8bf frag[32];
#pragma unroll
    for (int e = -L; e < 32; ++e) {
        const int f = e + L;
        if (f < 32) {
            const int n = f >> 1;
            if ((f & 1) == 0) {
                if (HAS_PV) {
                    const int s = n >> 2, db = n & 3;
                    v4s lo = __builtin_amdgcn_ds_read_tr16_b64_v4i16(
                        (__attribute__((address_space(3))) v4s*)(smem + st.voff[db] + VS * 16384 + s * 4096));
                    v4s hi = __builtin_amdgcn_ds_read_tr16_b64_v4i16(
                        (__attribute__((address_space(3))) v4s*)(smem + st.voff[db] + VS * 16384 + s * 4096 + 2048));
                    v8s vf = __builtin_shufflevector(lo, hi, 0, 1, 2, 3, 4, 5, 6, 7);
                    frag[f] = __builtin_bit_cast(v8bf, vf);
                }
            } else {
                if (HAS_QK) {
                    const int kb = n >> 3, i = n & 7;
                    frag[f] = *(const v8bf*)(smem + st.koff[i] + KS * 16384 + kb * 8192);
                }
            }
        }
        if (e >= 0) {
            const int n = e >> 1;
            if ((e & 1) == 0) {
                if (HAS_PV) {
                    const int s = n >> 2, db = n & 3;
                    st.oacc[db] = MFMA32(frag[e], __builtin_bit_cast(v8bf, st.pf[s]), st.oacc[db]);
                }
            } else {
                if (HAS_QK) {
                    const int kb = n >> 3, i = n & 7;
                    if (i == 0) {
                        if (FIRST_QK) {
                            v16f z;
#pragma unroll
                            for (int u = 0; u < 16; ++u) z[u] = 0.f;
                            st.sacc[kb] = MFMA32(frag[e], st.qf[i], z);
                        } else {
                            st.sacc[kb] = MFMA32(frag[e], st.qf[i], st.negm);
                        }
                    } else {
                        st.sacc[kb] = MFMA32(frag[e], st.qf[i], st.sacc[kb]);
                    }
                }
            }
        }
        SBAR();
    }
}

#define RESC_THR 8.0f
template <bool FIRST, class F>
DI void s_phase(AttnState& st, F&& dma) {
    float mx0 = fmaxf(fmaxf(st.sacc[0][0], st.sacc[0][1]), st.sacc[0][2]);
    float mx1 = fmaxf(fmaxf(st.sacc[1][0], st.sacc[1][1]), st.sacc[1][2]);
#pragma unroll
    for (int i = 3; i < 15; i += 2) {
        mx0 = fmaxf(fmaxf(mx0, st.sacc[0][i]), st.sacc[0][i + 1]);
        mx1 = fmaxf(fmaxf(mx1, st.sacc[1][i]), st.sacc[1][i + 1]);
    }
    mx0 = fmaxf(fmaxf(mx0, st.sacc[0][15]), st.sacc[1][15]);
    mx0 = fmaxf(mx0, mx1);
    {
        auto pr = __builtin_amdgcn_permlane32_swap(__float_as_uint(mx0), __float_as_uint(mx0), false, false);
        mx0 = fmaxf(__uint_as_float(pr[0]), __uint_as_float(pr[1]));
    }
    if (FIRST) {
#pragma unroll
        for (int kb = 0; kb < 2; ++kb)
#pragma unroll
            for (int i = 0; i < 16; ++i) st.sacc[kb][i] = fadd_s(st.sacc[kb][i], -mx0);
        st.m = mx0;
#pragma unroll
        for (int u = 0; u < 16; ++u) st.negm[u] = -mx0;
    } else if (__builtin_amdgcn_ballot_w64(mx0 > RESC_THR) != 0) {
        const float delta = fmaxf(mx0, 0.f);
        const float alpha = exp2_nop(-delta);
#pragma unroll
        for (int kb = 0; kb < 2; ++kb)
#pragma unroll
            for (int i = 0; i < 16; ++i) st.sacc[kb][i] = fadd_s(st.sacc[kb][i], -delta);
#pragma unroll
        for (int db = 0; db < 4; ++db)
#pragma unroll
            for (int i = 0; i < 16; ++i) st.oacc[db][i] = fmul_s(st.oacc[db][i], alpha);
        st.l = fmul_s(st.l, alpha);
        st.m = fadd_s(st.m, delta);
#pragma unroll
        for (int u = 0; u < 16; ++u) st.negm[u] = -st.m;
    }
    float ls = 0.f;
    SBAR();
#pragma unroll
    for (int kb = 0; kb < 2; ++kb) {
#pragma unroll
        for (int i = 0; i < 16; i += 2) {
            const float p0 = __builtin_amdgcn_exp2f(st.sacc[kb][i]);
            const float p1 = __builtin_amdgcn_exp2f(st.sacc[kb][i + 1]);
            ls += p0; ls += p1;
            st.pf[2 * kb + (i >> 3)][(i & 7) >> 1] = pk2(p0, p1);
            if ((i & 2) != 0) { SBAR(); dma(kb * 4 + (i >> 2)); SBAR(); }
        }
    }
    st.l += ls;
    asm volatile("" : "+v"(st.l));
#pragma unroll
    for (int s = 0; s < 4; ++s) asm volatile("" : "+v"(st.pf[s]));
}

__global__ __launch_bounds__(512, 2) void attn_kernel(const bf16_t* __restrict__ q, const bf16_t* __restrict__ kc,
                                                      const bf16_t* __restrict__ vc, bf16_t* __restrict__ o) {
    __shared__ __attribute__((aligned(16))) char smem[131072];
    const unsigned lds0 = (unsigned)(size_t)(__attribute__((address_space(3))) char*)smem;

    const int tid = threadIdx.x;
    const int lane = tid & 63;
    const int wid = __builtin_amdgcn_readfirstlane(tid >> 6);
    const int grp = wid >> 2;
    const int w = wid & 3;

    const int bid = blockIdx.x;
    const int xcd = bid & 7, j = bid >> 3;
    const int pair = xcd * 16 + (j >> 1);
    const int half = j & 1;
    const int b = pair >> 2, hkv = pair & 3;
    const int hq = hkv * 8 + half * 4 + w;

    const int r = lane & 31, h = lane >> 5;

    AttnState st;
#pragma unroll
    for (int i = 0; i < 8; ++i)
        st.koff[i] = grp * 65536 + r * 256 + 16 * ((2 * i + h) ^ (r & 15));
    {
        const int gi = lane >> 4, i16 = lane & 15, q4 = i16 >> 2, p4 = i16 & 3;
#pragma unroll
        for (int db = 0; db < 4; ++db)
            st.voff[db] = grp * 65536 + 32768 + (4 * (gi >> 1) + q4) * 256 + 64 * (db ^ q4) + 32 * (gi & 1) + 8 * p4;
    }

    const size_t kvbase = ((size_t)(b * PHKV + hkv) * PSK + grp * 1024) * PDH;
    const char* kg = (const char*)(kc + kvbase);
    const char* vg = (const char*)(vc + kvbase);
    unsigned kd[4];
#pragma unroll
    for (int pp = 0; pp < 4; ++pp)
        kd[pp] = (16 * w + (lane >> 4)) * 256 + 16 * ((lane & 15) ^ (4 * pp + (lane >> 4)));
    const unsigned vd = (16 * w + (lane >> 4)) * 256 + 16 * ((lane & 15) ^ ((lane >> 4) << 2));
    const unsigned ldsw = lds0 + grp * 65536 + w * 4096;

    auto dma_piece = [&](int i, const char* kb_, const char* vb_, int KS, int VS) {
        switch (i) {
            case 0: dma16<0>(ldsw + KS * 16384, kd[0], kb_); break;
            case 1: dma16<1024>(ldsw + KS * 16384, kd[1], kb_); break;
            case 2: dma16<2048>(ldsw + KS * 16384, kd[2], kb_); break;
            case 3: dma16<3072>(ldsw + KS * 16384, kd[3], kb_); break;
            case 4: dma16<0>(ldsw + 32768 + VS * 16384, vd, vb_); break;
            case 5: dma16<1024>(ldsw + 32768 + VS * 16384, vd, vb_); break;
            case 6: dma16<2048>(ldsw + 32768 + VS * 16384, vd, vb_); break;
            default: dma16<3072>(ldsw + 32768 + VS * 16384, vd, vb_); break;
        }
    };

    {
        const bf16_t* qp = q + (size_t)(b * PSQ + r) * PNQ + hq * PDH + 8 * h;
#pragma unroll
        for (int i = 0; i < 8; ++i) st.qf[i] = *(const v8bf*)(qp + 16 * i);
    }
    for (int i = 0; i < 4; ++i) dma_piece(i, kg, vg, 0, 0);
    for (int i = 4; i < 8; ++i) dma_piece(i, kg, vg, 0, 0);
    for (int i = 0; i < 4; ++i) dma_piece(i, kg + 16384, vg, 1, 0);

#pragma unroll
    for (int db = 0; db < 4; ++db)
#pragma unroll
        for (int i = 0; i < 16; ++i) st.oacc[db][i] = 0.f;
    st.m = 0.f;
    st.l = 0.f;

    WAIT_VM(8);
    BAR();
    if (grp) BAR();

#define STEP(t, PAR, FIRST, DK, DV, NW, LAST)                                                                     \
    do {                                                                                                          \
        s_phase<FIRST>(st, [&](int i) {                                                                           \
            if ((i < 4 && (DK)) || (i >= 4 && (DV)))                                                              \
                dma_piece(i, kg + (size_t)((t) + 2) * 16384, vg + (size_t)((t) + 1) * 16384, (PAR), 1 - (PAR));  \
        });                                                                                                       \
        SBAR();                                                                                                   \
        WAIT_VM(NW);                                                                                              \
        BAR_S();                                                                                                  \
        m_phase<true, !(LAST), (PAR), 1 - (PAR)>(st, smem);                                                       \
    } while (0)

    m_phase<false, true, 0, 0, true>(st, smem);
    BAR_M();
    STEP(0, 0, true, true, true, 8, false);  BAR_M();
    STEP(1, 1, false, true, true, 8, false); BAR_M();
    for (int t = 2; t < 14; t += 2) {
        STEP(t, 0, false, true, true, 8, false);      BAR_M();
        STEP(t + 1, 1, false, true, true, 8, false);  BAR_M();
    }
    STEP(14, 0, false, false, true, 4, false); BAR_M();
    STEP(15, 1, false, false, false, 0, true);

    {
        auto pr = __builtin_amdgcn_permlane32_swap(__float_as_uint(st.l), __float_as_uint(st.l), false, false);
        st.l = __uint_as_float(pr[0]) + __uint_as_float(pr[1]);
    }
    char* xw = smem + w * 16384;
    float* sx = (float*)(smem + 65536) + w * 128;
    if (grp) {
#pragma unroll
        for (int db = 0; db < 4; ++db)
#pragma unroll
            for (int g4 = 0; g4 < 4; ++g4) {
                v4f t4 = {st.oacc[db][4 * g4], st.oacc[db][4 * g4 + 1], st.oacc[db][4 * g4 + 2], st.oacc[db][4 * g4 + 3]};
                *(v4f*)(xw + (db * 4 + g4) * 1024 + lane * 16) = t4;
            }
        sx[lane] = st.m;
        sx[64 + lane] = st.l;
        WAIT_LGKM0();
        BAR();
        return;
    }
    BAR();
    BAR();
    {
        const float mB = sx[lane], lB = sx[64 + lane];
        const float mf = fmaxf(st.m, mB);
        const float aA = __builtin_amdgcn_exp2f(st.m - mf);
        const float aB = __builtin_amdgcn_exp2f(mB - mf);
        const float inv = 1.0f / (st.l * aA + lB * aB);
        const float fA = aA * inv, fB = aB * inv;
        char* stg = smem + 65536 + 16384 + w * (32 * 272);
#pragma unroll
        for (int db = 0; db < 4; ++db)
#pragma unroll
            for (int g4 = 0; g4 < 4; ++g4) {
                const v4f ob = *(const v4f*)(xw + (db * 4 + g4) * 1024 + lane * 16);
                v2u pk;
                pk[0] = pk2(st.oacc[db][4 * g4] * fA + ob[0] * fB, st.oacc[db][4 * g4 + 1] * fA + ob[1] * fB);
                pk[1] = pk2(st.oacc[db][4 * g4 + 2] * fA + ob[2] * fB, st.oacc[db][4 * g4 + 3] * fA + ob[3] * fB);
                *(v2u*)(stg + r * 272 + (32 * db + 8 * g4 + 4 * h) * 2) = pk;
            }
        WAIT_LGKM0();
        bf16_t* op = o + (size_t)(b * PSQ) * PNQ + hq * PDH;
#pragma unroll
        for (int it = 0; it < 8; ++it) {
            const int row = it * 4 + (lane >> 4), ch = lane & 15;
            *(v4u*)(op + (size_t)row * PNQ + ch * 8) = *(const v4u*)(stg + row * 272 + ch * 16);
        }
    }
}

template <int NT, int LDA, int LDB, bool OUT_F32>
__global__ __launch_bounds__(512, 2) void gemm_kernel(const bf16_t* __restrict__ A, const bf16_t* __restrict__ B,
                                                      void* __restrict__ Cout, int ldc, float oscale, int map_mode) {
    static_assert(NT % 4 == 0 && NT >= 8, "k-tiles per workgroup");
    __shared__ __attribute__((aligned(16))) char smem[65536 + 8 * 32 * 272 > 131072 ? 65536 + 8 * 32 * 272 : 131072];
    const unsigned lds0 = (unsigned)(size_t)(__attribute__((address_space(3))) char*)smem;
    const int tid = threadIdx.x, lane = tid & 63;
    const int wid = __builtin_amdgcn_readfirstlane(tid >> 6);
    const int grp = wid >> 2, w = wid & 3, wm = w >> 1, wn = w & 1;
    const int r = lane & 31, h = lane >> 5;

    const int bid = blockIdx.x, xcd = bid & 7, j = bid >> 3;
    int tile_m, tile_n, ksp;
    if (map_mode == 0) { tile_n = xcd * 4 + (j & 3); tile_m = j >> 2; ksp = 0; }
    else               { ksp = xcd & 3; tile_n = (xcd >> 2) * 4 + (j & 3); tile_m = j >> 2; }
    const int m0 = tile_m * 128, n0 = tile_n * 128, k0 = ksp * NT * 64;

    unsigned aoff[4], boff[2];
#pragma unroll
    for (int ik = 0; ik < 4; ++ik) aoff[ik] = (wm * 64 + r) * 128 + 16 * ((2 * ik + h) ^ ((r >> 1) & 7));
    {
        const int gi = lane >> 4, i16 = lane & 15, q4 = i16 >> 2, p4 = i16 & 3;
#pragma unroll
        for (int nb = 0; nb < 2; ++nb)
            boff[nb] = 16384 + (8 * (gi >> 1) + q4) * 256 + 64 * ((2 * wn + nb) ^ q4) + 32 * (gi & 1) + 8 * p4;
    }
    unsigned ao[4], bo[4];
#pragma unroll
    for (int pp = 0; pp < 4; ++pp) {
        const int ra = 8 * (4 * w + pp) + (lane >> 3);
        ao[pp] = ra * LDA * 2 + 16 * ((lane & 7) ^ ((4 * pp + (lane >> 4)) & 7)) - 1024 * pp;
        const int rb = 4 * (4 * w + pp) + (lane >> 4);
        bo[pp] = rb * LDB * 2 + 16 * ((lane & 15) ^ ((lane >> 4) << 2)) - 1024 * pp;
    }
    const char* ag = (const char*)(A + (size_t)m0 * LDA + k0);
    const char* bg = (const char*)(B + (size_t)k0 * LDB + n0);
    const unsigned ldw = lds0 + w * 4096;

    auto dma_tile = [&](int t, int slot) {
        const char* pa = ag + (size_t)t * 128;
        const char* pb = bg + (size_t)t * 64 * LDB * 2;
        const unsigned ma = ldw + slot * 32768, mb = ma + 16384;
        dma16d<0>(ma, ao[0], pa); dma16d<1024>(ma, ao[1], pa); dma16d<2048>(ma, ao[2], pa); dma16d<3072>(ma, ao[3], pa);
        dma16d<0>(mb, bo[0], pb); dma16d<1024>(mb, bo[1], pb); dma16d<2048>(mb, bo[2], pb); dma16d<3072>(mb, bo[3], pb);
    };

    v16f acc[2][2];
#pragma unroll
    for (int nb = 0; nb < 2; ++nb)
#pragma unroll
        for (int mb = 0; mb < 2; ++mb)
#pragma unroll
            for (int i = 0; i < 16; ++i) acc[nb][mb][i] = 0.f;

    auto c_phase = [&](auto slc) {
        constexpr int SL = decltype(slc)::value;
        v8bf af[4][2], bf[4][2];
#pragma unroll
        for (int g = -2; g < 4; ++g) {
            const int gl = g + 2;
            if (gl < 4) {
#pragma unroll
                for (int mb = 0; mb < 2; ++mb) af[gl][mb] = *(const v8bf*)(smem + aoff[gl] + SL * 32768 + mb * 4096);
#pragma unroll
                for (int nb = 0; nb < 2; ++nb) {
                    v4s lo = __builtin_amdgcn_ds_read_tr16_b64_v4i16(
                        (__attribute__((address_space(3))) v4s*)(smem + boff[nb] + SL * 32768 + gl * 4096));
                    v4s hi = __builtin_amdgcn_ds_read_tr16_b64_v4i16(
                        (__attribute__((address_space(3))) v4s*)(smem + boff[nb] + SL * 32768 + gl * 4096 + 1024));
                    bf[gl][nb] = __builtin_bit_cast(v8bf, __builtin_shufflevector(lo, hi, 0, 1, 2, 3, 4, 5, 6, 7));
                }
            }
            SBAR();
            if (g >= 0) {
#pragma unroll
                for (int nb = 0; nb < 2; ++nb)
#pragma unroll
                    for (int mb = 0; mb < 2; ++mb) { acc[nb][mb] = MFMA32(bf[g][nb], af[g][mb], acc[nb][mb]); SBAR(); }
            }
        }
    };
    using I0 = std::integral_constant<int, 0>; using I1 = std::integral_constant<int, 1>;
    using I2 = std::integral_constant<int, 2>; using I3 = std::integral_constant<int, 3>;

    dma_tile(grp, grp);
    dma_tile(grp + 2, (grp + 2) & 3);
    WAIT_VM(8);
    BAR();
    if (grp) BAR();

#define GSTEP(SLC, SLN, jj, DM, NW)                                                      \
    do {                                                                                 \
        c_phase(SLC{});                                                                  \
        BAR();                                                                           \
        if (DM) dma_tile(t0 + 2 * (jj) + 4, SLC::value);                                 \
        SBAR();                                                                          \
        WAIT_VM(NW);                                                                     \
        BAR();                                                                           \
    } while (0)

#define GLOOP(SA, SB)                                                                    \
    do {                                                                                 \
        const int t0 = grp;                                                              \
        for (int jj = 0; jj < NT / 2 - 2; jj += 2) {                                     \
            GSTEP(SA, SB, jj, true, 8);                                                  \
            GSTEP(SB, SA, jj + 1, true, 8);                                              \
        }                                                                                \
        GSTEP(SA, SB, 0, false, 0);                                                      \
        c_phase(SB{});                                                                   \
    } while (0)

    if (grp == 0) GLOOP(I0, I2); else GLOOP(I1, I3);

    BAR();
    if (grp == 0) BAR();
    constexpr int RB = OUT_F32 ? 256 : 128;
    constexpr int RS = RB + 16;
    constexpr int LPR = RB / 16;
    auto epilogue = [&](auto mbc) {
        constexpr int MB = decltype(mbc)::value;
        float* xo = (float*)(smem + (grp * 4 + w) * 8192);
        const float* xi = (const float*)(smem + ((1 - grp) * 4 + w) * 8192);
#pragma unroll
        for (int nb = 0; nb < 2; ++nb)
#pragma unroll
            for (int g4 = 0; g4 < 4; ++g4) {
                v4f t4 = {acc[nb][1 - MB][4 * g4], acc[nb][1 - MB][4 * g4 + 1], acc[nb][1 - MB][4 * g4 + 2], acc[nb][1 - MB][4 * g4 + 3]};
                *(v4f*)(xo + (nb * 4 + g4) * 256 + lane * 4) = t4;
            }
        WAIT_LGKM0();
        BAR();
        char* stg = smem + 65536 + wid * (32 * RS);
#pragma unroll
        for (int nb = 0; nb < 2; ++nb)
#pragma unroll
            for (int g4 = 0; g4 < 4; ++g4) {
                const v4f o4 = *(const v4f*)(xi + (nb * 4 + g4) * 256 + lane * 4);
                const float c0 = acc[nb][MB][4 * g4] + o4[0], c1 = acc[nb][MB][4 * g4 + 1] + o4[1];
                const float c2 = acc[nb][MB][4 * g4 + 2] + o4[2], c3 = acc[nb][MB][4 * g4 + 3] + o4[3];
                const int col = nb * 32 + 8 * g4 + 4 * h;
                if (OUT_F32) {
                    v4f t4 = {c0, c1, c2, c3};
                    *(v4f*)(stg + r * RS + col * 4) = t4;
                } else {
                    v2u pk; pk[0] = pk2(c0 * oscale, c1 * oscale); pk[1] = pk2(c2 * oscale, c3 * oscale);
                    *(v2u*)(stg + r * RS + col * 2) = pk;
                }
            }
        WAIT_LGKM0();
        const int mrow = m0 + wm * 64 + MB * 32;
#pragma unroll
        for (int it = 0; it < 32 * LPR / 64; ++it) {
            const int row = it * (64 / LPR) + lane / LPR, ch = lane % LPR;
            const v4u d = *(const v4u*)(stg + row * RS + ch * 16);
            if (OUT_F32)
                *(v4u*)((float*)Cout + ((size_t)ksp * PM + mrow + row) * ldc + n0 + wn * 64 + ch * 4) = d;
            else
                *(v4u*)((bf16_t*)Cout + (size_t)(mrow + row) * ldc + n0 + wn * 64 + ch * 8) = d;
        }
    };
    if (grp == 0) epilogue(I0{}); else epilogue(I1{});
}

__global__ __launch_bounds__(256) void reduce4_kernel(const float* __restrict__ p, bf16_t* __restrict__ out) {
    const size_t i = ((size_t)blockIdx.x * 256 + threadIdx.x) * 8;
    const size_t n = (size_t)PM * PD;
    v4f a0 = *(const v4f*)(p + i), a1 = *(const v4f*)(p + i + 4);
#pragma unroll
    for (int s = 1; s < 4; ++s) { a0 += *(const v4f*)(p + s * n + i); a1 += *(const v4f*)(p + s * n + i + 4); }
    v4u pk; pk[0] = pk2(a0[0], a0[1]); pk[1] = pk2(a0[2], a0[3]); pk[2] = pk2(a1[0], a1[1]); pk[3] = pk2(a1[2], a1[3]);
    *(v4u*)(out + i) = pk;
}

extern "C" void kernel_launch(void* const* d_in, const int* in_sizes, int n_in,
                              void* d_out, int out_size, void* d_ws, size_t ws_size,
                              hipStream_t stream) {
    const bf16_t* x  = (const bf16_t*)d_in[0];
    const bf16_t* kc = (const bf16_t*)d_in[1];
    const bf16_t* vc = (const bf16_t*)d_in[2];
    const bf16_t* wq = (const bf16_t*)d_in[3];
    const bf16_t* wo = (const bf16_t*)d_in[4];
    bf16_t* out = (bf16_t*)d_out;
    bf16_t* qb = (bf16_t*)d_ws;
    bf16_t* ob = qb + (size_t)PM * PNQ;
    float* part = (float*)(ob + (size_t)PM * PNQ);

    gemm_kernel<16, PD, PNQ, false><<<256, 512, 0, stream>>>(x, wq, qb, PNQ, QSCALE, 0);
    attn_kernel<<<256, 512, 0, stream>>>(qb, kc, vc, ob);
    gemm_kernel<16, PNQ, PD, true><<<256, 512, 0, stream>>>(ob, wo, part, PD, 1.0f, 1);
    reduce4_kernel<<<PM * PD / 2048, 256, 0, stream>>>(part, out);
}
```
